```python
import torch

def _softmax(x, axis=-1):
    return torch.softmax(x, dim=axis)
def _gelu(x):
    return torch.nn.functional.gelu(x, approximate="tanh")
def _silu(x):
    return torch.nn.functional.silu(x)
def _sigmoid(x):
    return torch.sigmoid(x)
def _layernorm(x, axis=-1, eps=1e-5):
    m = x.mean(dim=axis, keepdim=True)
    v = x.var(dim=axis, keepdim=True, unbiased=False)
    return (x - m) / torch.sqrt(v + eps)
def _scatter_add(tgt, idx, src, axis=0):
    return tgt.clone().index_add_(axis, idx, src)
def _segment_sum(src, seg, n):
    out = torch.zeros((n,) + src.shape[1:], dtype=src.dtype)
    return out.index_add_(0, seg, src)
def _pad(x, pw):
    flat = tuple(v for pair in reversed(pw) for v in pair)
    return torch.nn.functional.pad(x, flat)

kernel_name = 'concat_heads__gqa_block_cached__gqa_block_cached__near_ridge'

def setup_inputs():

    _w = (lambda a: (lambda t: t.to(torch.bfloat16) if t.is_floating_point() else t)(torch.as_tensor(a)))
    def _A_setup():
        B, Sq, D = 16, 96, 640
        Hq, Hkv, Sk, Dh = 32, 32, 512, 192
        assert Hq % Hkv == 0, (Hq, Hkv)
        return {
            "x": _w((lambda *s: torch.randn(*s, dtype=torch.bfloat16) * 0.8)(B, Sq, D)),
            "k_cache": _w((lambda *s: torch.randn(*s, dtype=torch.bfloat16) * 0.8)(B, Hkv, Sk, Dh)),
            "v_cache": _w((lambda *s: torch.randn(*s, dtype=torch.bfloat16) * 0.05)(B, Hkv, Sk, Dh)),
            "wq": _w((lambda *s: torch.randn(*s, dtype=torch.bfloat16) * 0.8)(D, Hq * Dh) * (D ** -0.5)),
            "wo": _w((lambda *s: torch.randn(*s, dtype=torch.bfloat16) * 0.05)(Hq * Dh, D) * ((Hq * Dh) ** -0.5)),
        }
    _A_in = _A_setup()

    def _B_setup():
        B, Sq, D = 16, 96, 640
        Hq, Hkv, Sk, Dh = 32, 32, 512, 192
        assert Hq % Hkv == 0, (Hq, Hkv)
        return {
            "x": _w((lambda *s: torch.randn(*s, dtype=torch.bfloat16) * 0.8)(B, Sq, D)),
            "k_cache": _w((lambda *s: torch.randn(*s, dtype=torch.bfloat16) * 0.8)(B, Hkv, Sk, Dh)),
            "v_cache": _w((lambda *s: torch.randn(*s, dtype=torch.bfloat16) * 0.05)(B, Hkv, Sk, Dh)),
            "wq": _w((lambda *s: torch.randn(*s, dtype=torch.bfloat16) * 0.8)(D, Hq * Dh) * (D ** -0.5)),
            "wo": _w((lambda *s: torch.randn(*s, dtype=torch.bfloat16) * 0.05)(Hq * Dh, D) * ((Hq * Dh) ** -0.5)),
        }
    _B_in = _B_setup()

    return {
        "A_x": _A_in["x"],
        "A_k_cache": _A_in["k_cache"],
        "A_v_cache": _A_in["v_cache"],
        "A_wq": _A_in["wq"],
        "A_wo": _A_in["wo"],
        "B_x": _B_in["x"],
        "B_k_cache": _B_in["k_cache"],
        "B_v_cache": _B_in["v_cache"],
        "B_wq": _B_in["wq"],
        "B_wo": _B_in["wo"],
    }

def reference(A_x, A_k_cache, A_v_cache, A_wq, A_wo, B_x, B_k_cache, B_v_cache, B_wq, B_wo):
    def _A_ref(x, k_cache, v_cache, wq, wo):
        B, Sq, D = x.shape
        Hq, Dh = 32, 192
        Hkv = k_cache.shape[1]
        rep = Hq // Hkv
        q = (lambda x, *d: x.permute(*d))((x @ wq).reshape(B, Sq, Hq, Dh), 0, 2, 1, 3)
        k = (lambda x, n, axis: torch.repeat_interleave(x, n, dim=axis))(k_cache, rep, axis=1)
        v = (lambda x, n, axis: torch.repeat_interleave(x, n, dim=axis))(v_cache, rep, axis=1)
        scores = torch.einsum("bhqd,bhkd->bhqk", q, k) * (Dh ** -0.5)
        p = _softmax(scores, axis=-1)
        o = torch.einsum("bhqk,bhkd->bhqd", p, v)
        return (lambda x, *d: x.permute(*d))(o, 0, 2, 1, 3).reshape(B, Sq, Hq * Dh) @ wo

    def _B_ref(x, k_cache, v_cache, wq, wo):
        B, Sq, D = x.shape
        Hq, Dh = 32, 192
        Hkv = k_cache.shape[1]
        rep = Hq // Hkv
        q = (lambda x, *d: x.permute(*d))((x @ wq).reshape(B, Sq, Hq, Dh), 0, 2, 1, 3)
        k = (lambda x, n, axis: torch.repeat_interleave(x, n, dim=axis))(k_cache, rep, axis=1)
        v = (lambda x, n, axis: torch.repeat_interleave(x, n, dim=axis))(v_cache, rep, axis=1)
        scores = torch.einsum("bhqd,bhkd->bhqk", q, k) * (Dh ** -0.5)
        p = _softmax(scores, axis=-1)
        o = torch.einsum("bhqk,bhkd->bhqd", p, v)
        return (lambda x, *d: x.permute(*d))(o, 0, 2, 1, 3).reshape(B, Sq, Hq * Dh) @ wo

    a = _A_ref(A_x, A_k_cache, A_v_cache, A_wq, A_wo)
    b = _B_ref(B_x, B_k_cache, B_v_cache, B_wq, B_wo)
    return torch.concatenate([a, b], axis=-1)
```

```cpp
#include <hip/hip_runtime.h>
#include <hip/hip_bf16.h>
#include <stdint.h>

constexpr int NB=16, SQ=96, DM=640, NH=32, SK=512, DH=192, HD=NH*DH;
typedef unsigned short u16;
__device__ __forceinline__ float bf2f(u16 x){ return __uint_as_float(((unsigned)x)<<16); }
__device__ __forceinline__ u16 f2bf(float f){ __bf16 b=(__bf16)f; return __builtin_bit_cast(u16,b); }

__global__ void gemm_naive(const u16* __restrict__ A, const u16* __restrict__ B, u16* __restrict__ C, int M,int N,int K,int ldc){
  __shared__ float As[32][33], Bs[32][33];
  int tx=threadIdx.x&31, ty=threadIdx.x>>5;
  int row0=blockIdx.y*32, col0=blockIdx.x*32;
  float acc[4]={0,0,0,0};
  for(int k0=0;k0<K;k0+=32){
    for(int i=0;i<4;i++){ int r=ty+8*i; As[r][tx]=bf2f(A[(size_t)(row0+r)*K+k0+tx]); Bs[r][tx]=bf2f(B[(size_t)(k0+r)*N+col0+tx]); }
    __syncthreads();
    for(int k=0;k<32;k++){ float b=Bs[k][tx]; for(int i=0;i<4;i++) acc[i]+=As[ty+8*i][k]*b; }
    __syncthreads();
  }
  for(int i=0;i<4;i++) C[(size_t)(row0+ty+8*i)*ldc+col0+tx]=f2bf(acc[i]);
}
__global__ void attn_naive(const u16* __restrict__ Q, const u16* __restrict__ Kc, const u16* __restrict__ Vc, u16* __restrict__ O){
  __shared__ float q[DH], p[SK], red[256];
  int qi=blockIdx.x, h=blockIdx.y, b=blockIdx.z, t=threadIdx.x;
  if(t<DH) q[t]=bf2f(Q[((size_t)(b*SQ+qi))*HD+h*DH+t]);
  __syncthreads();
  const u16* Kb=Kc+((size_t)(b*NH+h))*SK*DH; const u16* Vb=Vc+((size_t)(b*NH+h))*SK*DH;
  float m=-1e30f;
  for(int k=t;k<SK;k+=256){ float s=0; for(int d=0;d<DH;d++) s+=q[d]*bf2f(Kb[(size_t)k*DH+d]); s*=0.07216878364870322f; p[k]=s; m=fmaxf(m,s);}
  red[t]=m; __syncthreads();
  for(int s=128;s>0;s>>=1){ if(t<s) red[t]=fmaxf(red[t],red[t+s]); __syncthreads(); }
  m=red[0]; __syncthreads();
  float l=0; for(int k=t;k<SK;k+=256){ float e=__expf(p[k]-m); p[k]=e; l+=e; }
  red[t]=l; __syncthreads();
  for(int s=128;s>0;s>>=1){ if(t<s) red[t]+=red[t+s]; __syncthreads(); }
  l=red[0];
  if(t<DH){ float o=0; for(int k=0;k<SK;k++) o+=p[k]*bf2f(Vb[(size_t)k*DH+t]); O[((size_t)(b*SQ+qi))*HD+h*DH+t]=f2bf(o/l); }
}

extern "C" void kernel_launch(void* const* d_in, const int* in_sizes, int n_in, void* d_out, int out_size, void* d_ws, size_t ws_size, hipStream_t stream) {
  u16* ws=(u16*)d_ws; u16* Q=ws; u16* O=ws+(size_t)NB*SQ*HD;
  for(int br=0;br<2;br++){
    const u16* x=(const u16*)d_in[br*5+0]; const u16* k=(const u16*)d_in[br*5+1]; const u16* v=(const u16*)d_in[br*5+2];
    const u16* wq=(const u16*)d_in[br*5+3]; const u16* wo=(const u16*)d_in[br*5+4];
    gemm_naive<<<dim3(HD/32,NB*SQ/32),256,0,stream>>>(x,wq,Q,NB*SQ,HD,DM,HD);
    attn_naive<<<dim3(SQ,NH,NB),256,0,stream>>>(Q,k,v,O);
    gemm_naive<<<dim3(DM/32,NB*SQ/32),256,0,stream>>>(O,wo,(u16*)d_out+br*DM,NB*SQ,DM,HD,2*DM);
  }
}
```

```cpp
#include <hip/hip_runtime.h>
#include <hip/hip_bf16.h>
#include <stdint.h>
#include <utility>

typedef __bf16 bf16x2 __attribute__((ext_vector_type(2)));
typedef __bf16 bf16x4 __attribute__((ext_vector_type(4)));
typedef __bf16 bf16x8 __attribute__((ext_vector_type(8)));
typedef float f32x2 __attribute__((ext_vector_type(2)));
typedef float f32x4 __attribute__((ext_vector_type(4)));
typedef unsigned u32x2 __attribute__((ext_vector_type(2)));
typedef unsigned u32x4 __attribute__((ext_vector_type(4)));
#define LAS __attribute__((address_space(3)))
#define DI __device__ __forceinline__
typedef __amdgpu_buffer_rsrc_t rsrc_t;

constexpr int NB = 16, SQ = 96, DM = 640, NH = 32, SK = 512, DH = 192, HD = NH * DH;
#define WQ_NT false
#define X_NT false
#define K2_WO_NT false
#define O_NT 1
#define DMA_MODE 1
#define KV_NT true
#define COLD_NT 0
#define WO_WARM 1
#define LPRIO 3
#define WARM 1
#define STAG 0
#define STAG_MASK 1
#define W0_NT 0
#define TOFF 26
#define LA 4
#define PRIO_MODE 0
#define QSL 6
#define NUNITS 2
constexpr unsigned TILE = 12288;

template <int... Is, class F> DI void sfor_impl(std::integer_sequence<int, Is...>, F&& f) { (f(std::integral_constant<int, Is>{}), ...); }
template <int N, class F> DI void sfor(F&& f) { sfor_impl(std::make_integer_sequence<int, N>{}, f); }

DI unsigned pk2(float lo, float hi) { f32x2 v = {lo, hi}; bf16x2 b = __builtin_convertvector(v, bf16x2); return __builtin_bit_cast(unsigned, b); }
DI rsrc_t mk_rsrc(const void* p, unsigned bytes) { return __builtin_amdgcn_make_buffer_rsrc((void*)p, (short)0, (int)bytes, 0x00020000); }
template <bool NT> DI void dma16(rsrc_t r, unsigned voff, unsigned soff, unsigned lds) {
  if constexpr (NT) asm volatile("s_mov_b32 m0, %0\n\ts_nop 0\n\tbuffer_load_dwordx4 %1, %2, %3 offen nt lds" ::"s"(lds), "v"(voff), "s"(r), "s"(soff) : "memory");
  else              asm volatile("s_mov_b32 m0, %0\n\ts_nop 0\n\tbuffer_load_dwordx4 %1, %2, %3 offen lds" ::"s"(lds), "v"(voff), "s"(r), "s"(soff) : "memory");
}
#define WARM_NT 0
DI void warm64(rsrc_t r, unsigned voff, unsigned soff, unsigned lds) {
  if (WARM_NT) asm volatile("s_mov_b32 m0, %0\n\ts_nop 0\n\tbuffer_load_dword %1, %2, %3 offen nt lds" ::"s"(lds), "v"(voff), "s"(r), "s"(soff) : "memory");
  else asm volatile("s_mov_b32 m0, %0\n\ts_nop 0\n\tbuffer_load_dword %1, %2, %3 offen lds" ::"s"(lds), "v"(voff), "s"(r), "s"(soff) : "memory");
}
template <int N> DI void wait_bar() { asm volatile("s_waitcnt vmcnt(%0) lgkmcnt(0)\n\ts_barrier" ::"i"(N) : "memory"); }
DI void lgk_bar() {
  asm volatile("" ::: "memory");
  __builtin_amdgcn_s_waitcnt(0xC07F);
  __builtin_amdgcn_s_barrier();
  asm volatile("" ::: "memory");
}
DI bf16x8 lds128(LAS char* L, unsigned a) { return *(LAS bf16x8*)(L + a); }
DI bf16x8 ldstr(LAS char* L, unsigned a0, unsigned a1) {
  bf16x4 lo = __builtin_amdgcn_ds_read_tr16_b64_v4bf16((LAS bf16x4*)(L + a0));
  bf16x4 hi = __builtin_amdgcn_ds_read_tr16_b64_v4bf16((LAS bf16x4*)(L + a1));
  return __builtin_shufflevector(lo, hi, 0, 1, 2, 3, 4, 5, 6, 7);
}
DI float max3f(float a, float b, float c) { return __builtin_elementwise_maximum(__builtin_elementwise_maximum(a, b), c); }
DI float rowred_max(float x) {
  auto a = __builtin_amdgcn_permlane16_swap(__float_as_uint(x), __float_as_uint(x), false, false);
  x = __builtin_elementwise_maximum(__uint_as_float(a[0]), __uint_as_float(a[1]));
  auto b = __builtin_amdgcn_permlane32_swap(__float_as_uint(x), __float_as_uint(x), false, false);
  return __builtin_elementwise_maximum(__uint_as_float(b[0]), __uint_as_float(b[1]));
}
DI float rowred_sum(float x) {
  auto a = __builtin_amdgcn_permlane16_swap(__float_as_uint(x), __float_as_uint(x), false, false);
  x = __uint_as_float(a[0]) + __uint_as_float(a[1]);
  auto b = __builtin_amdgcn_permlane32_swap(__float_as_uint(x), __float_as_uint(x), false, false);
  return __uint_as_float(b[0]) + __uint_as_float(b[1]);
}
#define SB() __builtin_amdgcn_sched_barrier(0)
#define MFMA(a, b, c) __builtin_amdgcn_mfma_f32_16x16x32_bf16(a, b, c, 0, 0, 0)

constexpr size_t OFRAG_UNIT = 36 * 512;

#define STAMP(i)
#define STAMP2(i)
#define WT_BEGIN()
#define WT_END(acc)
#define WT_STORE(i, v)
#define WT_DECL()
struct K1Args { const char* x[2]; const char* k[2]; const char* v[2]; const char* wq[2]; const char* wo[2]; char* ofrag; unsigned* cnt; };

constexpr int NEV = 30;
constexpr int issued(int e) {
  if (e < 0) return 10;
  if (e <= 7) return 13 + 3 * e;
  if (e <= 9) return 37;
  if (e == 10 || e == 11) return 40;
  if (e <= 19) return 40 + 2 * (e - 11);
  if (e == 20) return 56;
  if (e <= 23) return 56 + 2 * (e - 20);
  if (e == 24) return 62;
  if (e == 25) return 65;
  if (e == 26) return 68;
  if (e == 27) return 70;
  return 72;
}
constexpr int kslot(int n) { const int r = n % 10; return r < 4 ? r : (r < 7 ? r + 3 : r - 3); }
constexpr int needpos(int e) {
  if (e < 10) return 3 * e + 2 < 6 ? 3 * e + 2 : 3 * e + 2 + 4;
  if (e >= 11 && e <= 18) { const int n = 2 * (e - 11) + 1; return n < 4 ? 6 + n : 30 + n; }
  if (e >= 20 && e <= 27) return 30 + 17 + 2 * (e - 20);
  return -1;
}
constexpr int imin(int a, int b) { return a < b ? a : b; }
constexpr int allowed(int e, bool last) { return needpos(e) < 0 ? 63 : imin(63, 3 * ((last ? imin(issued(e - 1), 62) : issued(e - 1)) - needpos(e) - 1)); }

constexpr unsigned AUX = 10 * TILE;
constexpr unsigned MISC = 13 * TILE;
constexpr unsigned K1_LDS = 13 * TILE + 4096;

__global__ __launch_bounds__(768, 3) void k1_attn(K1Args A) {
  extern __shared__ __attribute__((aligned(16))) char lds_raw[];
  LAS char* L = (LAS char*)lds_raw;
  const unsigned tid = threadIdx.x, lane = tid & 63;
  const unsigned w12 = __builtin_amdgcn_readfirstlane(tid >> 6);
  const unsigned bid = blockIdx.x, xcd = bid & 7, idx = bid >> 3;
  const unsigned br = xcd >> 2, h = 8 * (xcd & 3) + (idx & 7), b0 = idx >> 3;
  unsigned S[10];
#pragma unroll
  for (int i = 0; i < 10; ++i) S[i] = i * TILE;
  auto rotate = [&]() {
    const unsigned t0 = S[0], t1 = S[1], t2 = S[2], t3 = S[3], t4 = S[4], t5 = S[5], t6 = S[6], t7 = S[7], t8 = S[8], t9 = S[9];
    S[4] = t2; S[5] = t3; S[6] = t7; S[7] = t8; S[8] = t9; S[9] = t4; S[0] = t5; S[1] = t6; S[2] = t0; S[3] = t1;
  };
  auto qslot = [&](int qi) -> unsigned { const int m = qi / 3, r = qi % 3; return m % 3 == 2 ? AUX + r * TILE : S[4 + 3 * (m % 3) + r]; };
  auto olane = [&]() { unsigned l = lane; asm volatile("" : "+v"(l)); return l; };

  if (w12 >= 8) {
    if (LPRIO) __builtin_amdgcn_s_setprio(LPRIO);
    const unsigned w = w12 - 8;
    const unsigned ldsb = (unsigned)(uintptr_t)L;
    if (bid < 64 && tid == 512) A.cnt[bid * 32] = 0;
    const char* xp = br ? A.x[1] : A.x[0];
    const char* wp = br ? A.wq[1] : A.wq[0];
    const char* kbase = (br ? A.k[1] : A.k[0]);
    const char* vbase = (br ? A.v[1] : A.v[0]);
    const rsrc_t rw = mk_rsrc(wp + h * DH * 2, DM * HD * 2 - h * DH * 2);
    unsigned voX[3], voW[3], voK[3], voV[3];
#pragma unroll
    for (int i = 0; i < 3; ++i) {
      const unsigned P = 3 * w + i, row = 8 * P + (lane >> 3);
      voX[i] = row * (DM * 2) + 16 * ((lane & 7) ^ ((row >> 1) & 7));
      const unsigned o = 64 * i + lane, r = 8 * w + o / 24, cp = o % 24;
      const unsigned fW = 2 * (((r >> 1) & 1) | (((r >> 3) & 1) << 1)), fK = (r >> 1) & 7, fV = 2 * ((r >> 1) & 3);
      voW[i] = r * (HD * 2) + 16 * (cp ^ fW);
      voK[i] = r * 384 + 16 * (cp ^ fK);
      voV[i] = r * 384 + 16 * (cp ^ fV);
    }
    const unsigned dst_w = ldsb + 3072 * w;
    rsrc_t rx = mk_rsrc(xp + (size_t)b0 * SQ * DM * 2, SQ * DM * 2);
    rsrc_t rk = mk_rsrc(kbase + (size_t)(b0 * NH + h) * (SK * DH * 2), SK * DH * 2);
    rsrc_t rv = mk_rsrc(vbase + (size_t)(b0 * NH + h) * (SK * DH * 2), SK * DH * 2);
    unsigned soX = 0, soW = 0, soK = 0, soV = 0;
    auto issueQ = [&](rsrc_t rxx, int i, unsigned slot, bool cold) {
      if (i % 3 == 1) {
        if (COLD_NT && cold) {
#pragma unroll
          for (int p = 0; p < 3; ++p) dma16<true>(rxx, voX[p], soX, dst_w + slot + 1024 * p);
        } else {
#pragma unroll
          for (int p = 0; p < 3; ++p) dma16<X_NT>(rxx, voX[p], soX, dst_w + slot + 1024 * p);
        }
        soX += 128;
      } else {
        if (COLD_NT && cold) {
#pragma unroll
          for (int p = 0; p < 3; ++p) dma16<true>(rw, voW[p], soW, dst_w + slot + 1024 * p);
        } else {
#pragma unroll
          for (int p = 0; p < 3; ++p) dma16<WQ_NT>(rw, voW[p], soW, dst_w + slot + 1024 * p);
        }
        soW += 32u * HD * 2;
      }
    };
    auto issueK = [&](rsrc_t rkk, unsigned slot) {
#pragma unroll
      for (int p = 0; p < 3; ++p) dma16<KV_NT>(rkk, voK[p], soK, dst_w + slot + 1024 * p);
      soK += TILE;
    };
    auto issueV = [&](unsigned slot) {
#pragma unroll
      for (int p = 0; p < 3; ++p) dma16<KV_NT>(rv, voV[p], soV, dst_w + slot + 1024 * p);
      soV += TILE;
    };
    if (STAG > 0) {
      const unsigned long long t0 = __builtin_amdgcn_s_memrealtime(), dl = (unsigned long long)STAG * (b0 & STAG_MASK);
      while (__builtin_amdgcn_s_memrealtime() - t0 < dl) __builtin_amdgcn_s_sleep(8);
    }
#pragma unroll
    for (int i = 0; i < 6; ++i) issueQ(rx, i, S[4 + i], true);
#pragma unroll
    for (int n = 0; n < 4; ++n) issueK(rk, S[n]);
    for (int un = 0; un < 4; ++un) {
      const bool last = un == 3;
      const unsigned bn = last ? b0 : b0 + 4 * (un + 1);
      const rsrc_t rxn = mk_rsrc(xp + (size_t)bn * SQ * DM * 2, SQ * DM * 2);
      const rsrc_t rkn = mk_rsrc(kbase + (size_t)(bn * NH + h) * (SK * DH * 2), SK * DH * 2);
      const rsrc_t rvn = mk_rsrc(vbase + (size_t)(bn * NH + h) * (SK * DH * 2), SK * DH * 2);
      sfor<NEV>([&](auto E) {
        constexpr int e = E;
        if constexpr (allowed(e, false) == allowed(e, true)) wait_bar<allowed(e, false)>();
        else { if (last) wait_bar<allowed(e, true)>(); else wait_bar<allowed(e, false)>(); }
        constexpr int lo = issued(e - 1), hi = issued(e);
        sfor<3>([&](auto D) {
          constexpr int pos = lo + D;
          if constexpr (pos < hi) {
            if constexpr (pos < 34) {
              constexpr int qi = pos - 4;
              issueQ(rx, qi, qslot(qi), un == 0);
            } else if constexpr (pos < 62) {
              constexpr int n = pos - 30;
              if constexpr (n < 16) issueK(rk, S[kslot(n)]); else issueV(S[kslot(n)]);
            } else if (!last) {
              if constexpr (pos < 68) {
                if constexpr (pos == 62) { soX = 0; soW = 0; asm volatile("" : "+s"(soX), "+s"(soW)); }
                issueQ(rxn, pos - 62, S[kslot(22 + pos - 62)], false);
              } else {
                if constexpr (pos == 68) { soK = 0; asm volatile("" : "+s"(soK)); }
                issueK(rkn, S[kslot(28 + pos - 68)]);
              }
            }
          }
        });
      });
      rotate();
      rx = rxn; rk = rkn; rv = rvn;
      soV = 0; asm volatile("" : "+s"(soV));
    }
    return;
  }

  const unsigned cw = w12, qh = cw >> 2, jj = cw & 3;
  const unsigned wscr = (unsigned)(uintptr_t)L + MISC + 3072 + 128 * 0;
  auto warm_x = [&](unsigned bb) {
    if (WARM && cw < 2) {
      const rsrc_t r = mk_rsrc((br ? A.x[1] : A.x[0]) + (size_t)bb * SQ * DM * 2, SQ * DM * 2);
      const unsigned l = olane(), line = 120 * (idx & 7) + 60 * cw + (l < 60 ? l : 59);
      warm64(r, line * 128, 0, wscr);
    }
  };
  auto warm_wq = [&]() {
    const rsrc_t r = mk_rsrc((br ? A.wq[1] : A.wq[0]) + h * DH * 2, DM * HD * 2 - h * DH * 2);
    const unsigned l = olane(), i = 60 * cw + (l < 60 ? l : 59), row = 160 * b0 + i / 3;
    warm64(r, row * (HD * 2) + 128 * (i % 3), 0, wscr);
  };
  auto warm_wo = [&]() {
    const rsrc_t r = mk_rsrc((br ? A.wo[1] : A.wo[0]) + (size_t)(8 * (xcd & 3) * DH) * (DM * 2), 8 * DH * DM * 2);
    const unsigned l = olane(), i = 480 * idx + 60 * cw + (l < 60 ? l : 59);
    warm64(r, i * 128, 0, wscr);
  };
  if (WARM && !COLD_NT) { warm_wq(); warm_x(b0); }
  const float C = 0.07216878364870322f * 1.4426950408889634f;
  LAS float* red = (LAS float*)(L + MISC);
  for (int un = 0; un < 4; ++un) {
    const unsigned b = b0 + 4 * un;
    STAMP(0);
    unsigned bW[3], bX0;
    {
      const unsigned l = olane(), c = l & 15, gg = l >> 4, a = c >> 2, p = l & 3;
      const unsigned r = 8 * gg + a, fW = 2 * (((r >> 1) & 1) | (((r >> 3) & 1) << 1));
#pragma unroll
      for (int e = 0; e < 3; ++e) { const unsigned ch = 2 * (3 * jj + e) + (p >> 1); bW[e] = 384 * r + 128 * (ch >> 3) + 16 * ((ch & 7) ^ fW) + 8 * (p & 1); }
      bX0 = 128 * (48 * qh + c) + 16 * (gg ^ ((c >> 1) & 7));
    }
    f32x4 qacc[3][3];
#pragma unroll
    for (int e = 0; e < 3; ++e)
#pragma unroll
      for (int t = 0; t < 3; ++t) qacc[e][t] = f32x4{0, 0, 0, 0};
    bf16x8 xd[3], wd[2];
    auto q_tail = [&]() {
#pragma unroll
      for (int f = 4; f < 6; ++f)
#pragma unroll
        for (int t = 0; t < 3; ++t) qacc[f % 3][t] = MFMA(wd[f - 4], xd[t], qacc[f % 3][t]);
    };
    sfor<10>([&](auto M) {
      constexpr int m = M;
      lgk_bar();
      const unsigned sw0 = qslot(3 * m), sx = qslot(3 * m + 1), sw1 = qslot(3 * m + 2);
      bf16x8 xf[3], wf[4], xn[3], wn[2];
      auto rdW = [&](int f) { const unsigned a = bW[f % 3] + (f < 3 ? sw0 : sw1); return ldstr(L, a, a + 1536); };
      wf[0] = rdW(0);
#pragma unroll
      for (int t = 0; t < 3; ++t) xf[t] = lds128(L, bX0 + sx + 2048 * t);
      wf[1] = rdW(1);
      SB();
      if constexpr (m > 0) q_tail();
      SB();
      sfor<4>([&](auto F) {
        constexpr int f = F;
        if constexpr (f + 2 < 4) wf[f + 2] = rdW(f + 2); else wn[f - 2] = rdW(f + 2);
        if constexpr (f == 0) {
#pragma unroll
          for (int t = 0; t < 3; ++t) xn[t] = lds128(L, (bX0 ^ 64) + sx + 2048 * t);
        }
#pragma unroll
        for (int t = 0; t < 3; ++t) qacc[f % 3][t] = MFMA(wf[f], f < 3 ? xf[t] : xn[t], qacc[f % 3][t]);
        SB();
      });
#pragma unroll
      for (int t = 0; t < 3; ++t) xd[t] = xn[t];
      wd[0] = wn[0]; wd[1] = wn[1];
    });
    q_tail();
    STAMP(1);
    bf16x8 qf[6][3];
    unsigned bK0;
    {
      const unsigned l = olane(), c = l & 15, gg = l >> 4;
      const unsigned fK = (c >> 1) & 7;
      bK0 = 384 * c + 16 * (gg ^ fK);
#pragma unroll
      for (int e = 0; e < 3; ++e) {
        const unsigned ch = 2 * (3 * jj + e) + (gg >> 1);
        const unsigned a = AUX + 18432 * qh + 384 * c + 128 * (ch >> 3) + 16 * ((ch & 7) ^ fK) + 8 * (gg & 1);
#pragma unroll
        for (int t = 0; t < 3; ++t)
          *(LAS u32x2*)(L + a + 6144 * t) = u32x2{pk2(qacc[e][t][0], qacc[e][t][1]), pk2(qacc[e][t][2], qacc[e][t][3])};
      }
      lgk_bar();
#pragma unroll
      for (int s = 0; s < 6; ++s)
#pragma unroll
        for (int t = 0; t < 3; ++t) qf[s][t] = lds128(L, AUX + 18432 * qh + 6144 * t + ((s & 1) ? (bK0 ^ 64) : bK0) + 128 * (s >> 1));
    }
    STAMP(2);
    if (un < 3) warm_x(b + 4);
    if (WARM && COLD_NT && un == 0) warm_wq();
    u32x2 sp[8][3];
    float mx[3] = {-3.0e38f, -3.0e38f, -3.0e38f};
    f32x4 sa[3];
    bf16x8 kd[2];
    auto k_tail = [&](auto T) {
      constexpr int t = T;
#pragma unroll
      for (int s = 4; s < 6; ++s)
#pragma unroll
        for (int q = 0; q < 3; ++q) sa[q] = MFMA(kd[s - 4], qf[s][q], sa[q]);
#pragma unroll
      for (int q = 0; q < 3; ++q) {
        mx[q] = max3f(max3f(sa[q][0], sa[q][1], mx[q]), sa[q][2], sa[q][3]);
        sp[t][q] = u32x2{pk2(sa[q][0], sa[q][1]), pk2(sa[q][2], sa[q][3])};
        asm volatile("" : "+v"(sp[t][q]), "+v"(mx[q]));
      }
    };
    sfor<8>([&](auto T) {
      constexpr int t = T;
      lgk_bar();
      const unsigned so = (jj >> 1) ? S[kslot(2 * t + 1)] : S[kslot(2 * t)];
      const unsigned k0 = bK0 + 6144 * (jj & 1) + so, k1 = k0 ^ 64;
      bf16x8 kf[4], kn[2];
      auto rdK = [&](int s) { return lds128(L, ((s & 1) ? k1 : k0) + 128 * (s >> 1)); };
      kf[0] = rdK(0); kf[1] = rdK(1);
      SB();
      if constexpr (t > 0) k_tail(std::integral_constant<int, (t > 0 ? t - 1 : 0)>{});
      SB();
#pragma unroll
      for (int q = 0; q < 3; ++q) sa[q] = f32x4{0, 0, 0, 0};
      sfor<4>([&](auto F) {
        constexpr int s = F;
        if constexpr (s + 2 < 4) kf[s + 2] = rdK(s + 2); else kn[s - 2] = rdK(s + 2);
#pragma unroll
        for (int q = 0; q < 3; ++q) sa[q] = MFMA(kf[s], qf[s][q], sa[q]);
        SB();
      });
      kd[0] = kn[0]; kd[1] = kn[1];
    });
    k_tail(std::integral_constant<int, 7>{});
    STAMP(3);
    const unsigned lsm = olane(), csm = lsm & 15;
#pragma unroll
    for (int q = 0; q < 3; ++q) {
      mx[q] = rowred_max(mx[q]);
      if (lsm < 16) red[cw * 48 + 16 * q + csm] = mx[q];
    }
    lgk_bar();
    float mc[3], s0[3], s1[3];
    auto expo = [&](int t, int q, int r) {
      const unsigned u = sp[t][q][r];
      const float p0 = __builtin_amdgcn_exp2f(fmaf(__uint_as_float(u << 16), C, mc[q]));
      const float p1 = __builtin_amdgcn_exp2f(fmaf(__uint_as_float(u & 0xffff0000u), C, mc[q]));
      s0[q] += p0; s1[q] += p1;
      asm volatile("" : "+v"(s0[q]), "+v"(s1[q]));
      sp[t][q][r] = pk2(p0, p1);
    };
    unsigned bV[3], pw, pr;
    {
      const unsigned gg = lsm >> 4, a = csm >> 2, p = lsm & 3;
      const unsigned rv_ = 4 * gg + a, fV = 2 * ((rv_ >> 1) & 3);
#pragma unroll
      for (int e = 0; e < 3; ++e) { const unsigned ch = 2 * (3 * jj + e) + (p >> 1); bV[e] = 384 * rv_ + 128 * (ch >> 3) + 16 * ((ch & 7) ^ fV) + 8 * (p & 1); }
      pr = AUX + 6144 * qh + 16 * lsm;
      pw = pr + 3072 * (jj >> 1) + 8 * (jj & 1);
    }
#pragma unroll
    for (int q = 0; q < 3; ++q) {
      const unsigned ri = 4 * qh * 48 + 16 * q + csm;
      const float m = max3f(max3f(red[ri], red[ri + 48], red[ri + 96]), red[ri + 144], mx[q]);
      mc[q] = -m * C; s0[q] = 0.f; s1[q] = 0.f;
      expo(0, q, 0); expo(0, q, 1);
      *(LAS u32x2*)(L + pw + 1024 * q) = sp[0][q];
    }
    STAMP(4);
    f32x4 oacc[3][3];
#pragma unroll
    for (int e = 0; e < 3; ++e)
#pragma unroll
      for (int t = 0; t < 3; ++t) oacc[e][t] = f32x4{0, 0, 0, 0};
    bf16x8 pd[3], vd[2];
    auto v_tail = [&]() {
#pragma unroll
      for (int f = 4; f < 6; ++f)
#pragma unroll
        for (int q = 0; q < 3; ++q) oacc[f % 3][q] = MFMA(vd[f - 4], pd[q], oacc[f % 3][q]);
    };
    sfor<8>([&](auto T) {
      constexpr int t = T, n = 16 + 2 * t;
      lgk_bar();
      bf16x8 pf[3], pn[3], vf[4], vn[2];
      auto rdV = [&](int f) { const unsigned a = bV[f % 3] + S[kslot(n + f / 3)]; return ldstr(L, a, a + 6144); };
#pragma unroll
      for (int q = 0; q < 3; ++q) pf[q] = lds128(L, pr + 12288 * (t & 1) + 1024 * q);
      vf[0] = rdV(0); vf[1] = rdV(1);
      SB();
      if constexpr (t == 3) { if (WO_WARM && un == 3) warm_wo(); }
      if constexpr (t > 0) v_tail();
      if constexpr (t < 7) { expo(t + 1, 0, 0); expo(t + 1, 0, 1); *(LAS u32x2*)(L + pw + 12288 * ((t + 1) & 1)) = sp[t + 1][0]; }
      SB();
      sfor<4>([&](auto F) {
        constexpr int f = F;
        if constexpr (f + 2 < 4) vf[f + 2] = rdV(f + 2); else vn[f - 2] = rdV(f + 2);
        if constexpr (f == 0) {
#pragma unroll
          for (int q = 0; q < 3; ++q) pn[q] = lds128(L, pr + 12288 * (t & 1) + 3072 + 1024 * q);
        }
#pragma unroll
        for (int q = 0; q < 3; ++q) oacc[f % 3][q] = MFMA(vf[f], f < 3 ? pf[q] : pn[q], oacc[f % 3][q]);
        if constexpr (t < 7) {
          expo(t + 1, 1 + (f >> 1), f & 1);
          if constexpr (f & 1) *(LAS u32x2*)(L + pw + 12288 * ((t + 1) & 1) + 1024 * (1 + (f >> 1))) = sp[t + 1][1 + (f >> 1)];
        }
        SB();
      });
#pragma unroll
      for (int q = 0; q < 3; ++q) pd[q] = pn[q];
      vd[0] = vn[0]; vd[1] = vn[1];
    });
    v_tail();
    STAMP(5);
    float ls[3];
    const unsigned l = olane();
#pragma unroll
    for (int q = 0; q < 3; ++q) {
      const float s = rowred_sum(s0[q] + s1[q]);
      if (l < 16) red[384 + cw * 48 + 16 * q + l] = s;
    }
    lgk_bar();
#pragma unroll
    for (int q = 0; q < 3; ++q) {
      const unsigned ri = 384 + 4 * qh * 48 + 16 * q + (l & 15);
      ls[q] = __builtin_amdgcn_rcpf((red[ri] + red[ri + 48]) + (red[ri + 96] + red[ri + 144]));
    }
#pragma unroll
    for (int e = 0; e < 3; ++e) {
      const unsigned u = 3 * jj + e;
      const unsigned a = AUX + ((u >> 1) * 6 + 3 * qh) * 1024 + 16 * l + 8 * (u & 1);
#pragma unroll
      for (int q = 0; q < 3; ++q)
        *(LAS u32x2*)(L + a + 1024 * q) = u32x2{pk2(oacc[e][q][0] * ls[q], oacc[e][q][1] * ls[q]), pk2(oacc[e][q][2] * ls[q], oacc[e][q][3] * ls[q])};
    }
    lgk_bar();
    {
      char* ob = A.ofrag + ((size_t)((br * 16 + b) * NH + h) * OFRAG_UNIT) * 2 + 16 * l + 1024 * cw;
      u32x4 o[5];
#pragma unroll
      for (int i = 0; i < 5; ++i) if (i < 4 || cw < 4) o[i] = *(LAS u32x4*)(L + AUX + 1024 * cw + 8192 * i + 16 * l);
#pragma unroll
      for (int i = 0; i < 5; ++i) if (i < 4 || cw < 4) {
        if (O_NT) __builtin_nontemporal_store(o[i], (u32x4*)(ob + 8192 * i)); else *(u32x4*)(ob + 8192 * i) = o[i];
      }
    }
    rotate();
    STAMP(6);
  }
}

#define K2_NBUF 4
#define K2_WARM 0
#define K2_PBF16 1
constexpr unsigned K2_STAGE = 45056;
constexpr unsigned K2_LDS = 3 * K2_STAGE + 256;
constexpr size_t WS_PART = (size_t)32 * 32 * OFRAG_UNIT * 2;
constexpr size_t PART_BYTES = (size_t)64 * 4 * 8 * 15 * 1024;
constexpr size_t WS_CNT = WS_PART + PART_BYTES;
struct K2Args { const char* wo[2]; char* ws; char* out; };
typedef __attribute__((address_space(1))) unsigned gu32;

#define K2_NL 4
__global__ __launch_bounds__(512 + 64 * K2_NL, 2 + K2_NL / 4) void k2_oproj(K2Args A) {
  extern __shared__ __attribute__((aligned(16))) char lds_raw[];
  LAS char* L = (LAS char*)lds_raw;
  const unsigned ldsb = (unsigned)(uintptr_t)L;
  const unsigned tid = threadIdx.x, lane = tid & 63;
  const unsigned w = __builtin_amdgcn_readfirstlane(tid >> 6);
  const unsigned mh = (w >> 2) & 1, nh = (w >> 1) & 1, qh = w & 1;
  const unsigned c16 = lane & 15, g = lane >> 4, a4 = c16 >> 2, p4 = lane & 3;
  const unsigned bid = blockIdx.x, xcd = bid & 7, idx = bid >> 3;
  const unsigned br = xcd >> 2, kq = xcd & 3, mb = idx >> 2, nb = idx & 3;
  const char* wo = (br ? A.wo[1] : A.wo[0]);
  STAMP2(0);
  f32x4 acc[5][3];
#pragma unroll
  for (int u = 0; u < 5; ++u)
#pragma unroll
    for (int q = 0; q < 3; ++q) acc[u][q] = f32x4{0, 0, 0, 0};
  if (w >= 8) {
    if (LPRIO) __builtin_amdgcn_s_setprio(LPRIO);
    const unsigned lw = w - 8;
    constexpr int NO = 24 / K2_NL, NWP = K2_NL == 4 ? 5 : 3;
    const unsigned go = br * 16 + 2 * mb + (lw * NO) / 12;
    const rsrc_t ro = mk_rsrc(A.ws + ((size_t)(go * NH + 8 * kq) * OFRAG_UNIT) * 2, 8 * OFRAG_UNIT * 2);
    const rsrc_t rw = mk_rsrc(wo + (size_t)(8 * kq * DH) * (DM * 2) + nb * 320, 8 * DH * DM * 2 - nb * 320);
    const unsigned voO = 16 * lane + 1024 * ((lw * NO) % 12), dstO = ldsb + 1024 * NO * lw;
    const unsigned pW0 = K2_NL == 4 ? 5 * lw : (lw < 4 ? 3 * lw : 12 + 2 * (lw - 4));
    unsigned voW[NWP];
#pragma unroll
    for (int p = 0; p < NWP; ++p) { const unsigned o = 64 * (pW0 + p) + lane, r = o / 20, cp = o % 20; voW[p] = r * (DM * 2) + 16 * (cp ^ (2 * ((r >> 2) & 1))); }
    const unsigned dstW = ldsb + 24576 + 1024 * pW0;
    unsigned soO = 0, soW = 0;
    auto stage_dma = [&](unsigned stg) {
#pragma unroll
      for (int p = 0; p < NO; ++p) dma16<false>(ro, voO + 1024 * p, soO, dstO + stg + 1024 * p);
#pragma unroll
      for (int p = 0; p < NWP; ++p) if (K2_NL == 4 || p < 2 || lw < 4) dma16<K2_WO_NT>(rw, voW[p], soW, dstW + stg + 1024 * p);
      soO += 12288; soW += 64 * DM * 2;
    };
    stage_dma(0); stage_dma(K2_STAGE); stage_dma(2 * K2_STAGE);
    unsigned free_stg = 0;
    constexpr int NP = NO + NWP;
    const bool shortw = K2_NL == 8 && lw >= 4;
    for (int st = 0; st < 24; ++st) {
      if (st == 0) { if (shortw) wait_bar<2 * (NP - 1)>(); else wait_bar<2 * NP>(); }
      else if (st < 23) { if (shortw) wait_bar<NP - 1>(); else wait_bar<NP>(); }
      else wait_bar<0>();
      if (st >= 1 && st + 2 < 24) { stage_dma(free_stg); free_stg = (free_stg == 2 * K2_STAGE) ? 0 : free_stg + K2_STAGE; }
    }
    __builtin_amdgcn_s_barrier();
  } else {
    const unsigned bB = 12288 * mh + 3072 * qh + 16 * lane;
    unsigned bA[2];
    {
      const unsigned r = 4 * g + a4, sgn = 32 * (g & 1);
      const unsigned be = 24576 + 320 * r + 8 * p4 + 160 * nh + sgn, bo = 24576 + 320 * r + 8 * p4 + 160 * nh - sgn;
      bA[0] = nh ? bo : be; bA[1] = nh ? be : bo;
    }
    u32x4 da[5], db[3];
    unsigned stc = 0;
    auto mm = [&](u32x4 (&fa)[5], u32x4 (&fb)[3]) {
#pragma unroll
      for (int u = 0; u < 5; ++u)
#pragma unroll
        for (int q = 0; q < 3; ++q) acc[u][q] = MFMA(__builtin_bit_cast(bf16x8, fa[u]), __builtin_bit_cast(bf16x8, fb[q]), acc[u][q]);
    };
    auto rd = [&](int i, u32x4 (&fa)[5], u32x4 (&fb)[3], unsigned ob, unsigned oa) {
      if (i == 0 || i == 2 || i == 3) { const int q = i == 0 ? 0 : i - 1; fb[q] = __builtin_bit_cast(u32x4, lds128(L, bB + ob + 1024 * q)); }
      else { const int u = i == 1 ? 0 : i - 3; const unsigned a = ((u & 1) ? bA[1] : bA[0]) + oa + 32 * u; fa[u] = __builtin_bit_cast(u32x4, ldstr(L, a, a + 5120)); }
    };
    auto half = [&](bool domm, u32x4 (&ma)[5], u32x4 (&mb_)[3], u32x4 (&ra)[5], u32x4 (&rb)[3], unsigned ob, unsigned oa) {
#pragma unroll
      for (int i = 0; i < 15; ++i) {
        if (domm) acc[i / 3][i % 3] = MFMA(__builtin_bit_cast(bf16x8, ma[i / 3]), __builtin_bit_cast(bf16x8, mb_[i % 3]), acc[i / 3][i % 3]);
        if (i < 8) rd(i, ra, rb, ob, oa);
        SB();
      }
    };
    for (int st = 0; st < 24; ++st) {
      lgk_bar();
      u32x4 fa[5], fb[3];
      half(st > 0, da, db, fa, fb, stc, stc);
      __builtin_amdgcn_s_waitcnt(0xC07F);
      SB();
      half(true, fa, fb, da, db, stc + 6144, stc + 10240);
      stc = (stc == 2 * K2_STAGE) ? 0 : stc + K2_STAGE;
    }
    lgk_bar();
    mm(da, db);
  }
  STAMP2(1);
  STAMP2(2);
  {
    const unsigned tg = (br * 8 + mb) * 4 + nb;
    constexpr unsigned TB = K2_PBF16 ? 512 : 1024;
    char* pbase = A.ws + WS_PART + (size_t)tg * (4 * 8 * 15 * TB);
    const rsrc_t rp = mk_rsrc(pbase, 4 * 8 * 15 * TB);
    const unsigned vo = (w * 15) * TB + (TB / 64) * lane;
#pragma unroll
    for (int i = 0; i < 15; ++i)
      if (w < 8 && (i & 3) != kq) {
        const f32x4 v = acc[i / 3][i % 3];
        if constexpr (K2_PBF16) __builtin_amdgcn_raw_buffer_store_b64(u32x2{pk2(v[0], v[1]), pk2(v[2], v[3])}, rp, vo + TB * i, kq * (8 * 15 * TB), 16);
        else __builtin_amdgcn_raw_buffer_store_b128(__builtin_bit_cast(u32x4, v), rp, vo + TB * i, kq * (8 * 15 * TB), 16);
      }
    asm volatile("s_waitcnt vmcnt(0)" ::: "memory");
    __builtin_amdgcn_s_barrier();
    STAMP2(3);
    gu32* cnt = (gu32*)(A.ws + WS_CNT + 128 * tg);
    if (tid == 0) {
      __hip_atomic_fetch_add(cnt, 1u, __ATOMIC_RELAXED, __HIP_MEMORY_SCOPE_AGENT);
      unsigned spins = 0;
      while (__hip_atomic_load(cnt, __ATOMIC_RELAXED, __HIP_MEMORY_SCOPE_AGENT) < 4u) { __builtin_amdgcn_s_sleep(2); if (++spins > (1u << 22)) break; }
    }
    asm volatile("s_waitcnt vmcnt(0)" ::: "memory");
    __builtin_amdgcn_s_barrier();
    asm volatile("" ::: "memory");
    STAMP2(4);
    if (w >= 8) return;
    const unsigned b = 2 * mb + mh;
    char* outp = A.out + ((size_t)(b * SQ + 48 * qh + c16) * (2 * DM) + br * DM + nb * 160 + nh * 80 + 4 * g) * 2;
    f32x4 own[4];
    u32x4 pf[4][3];
    u32x2 ph[4][3];
#pragma unroll
    for (int k = 0; k < 4; ++k) {
      own[k] = kq == 0 ? acc[(4 * k) / 3][(4 * k) % 3] : kq == 1 ? acc[(4 * k + 1) / 3][(4 * k + 1) % 3] : kq == 2 ? acc[(4 * k + 2) / 3][(4 * k + 2) % 3]
                                                                                                          : acc[(k < 3 ? 4 * k + 3 : 0) / 3][(k < 3 ? 4 * k + 3 : 0) % 3];
      const unsigned i = 4 * k + kq;
#pragma unroll
      for (int o = 0; o < 3; ++o) {
        const unsigned src = (kq + 1 + o) & 3;
        const unsigned off = i < 15 ? vo + TB * i : 4 * 8 * 15 * TB;
        if constexpr (K2_PBF16) ph[k][o] = __builtin_amdgcn_raw_buffer_load_b64(rp, off, src * (8 * 15 * TB), 16);
        else pf[k][o] = __builtin_amdgcn_raw_buffer_load_b128(rp, off, src * (8 * 15 * TB), 16);
      }
    }
#pragma unroll
    for (int k = 0; k < 4; ++k) {
      f32x4 s = own[k];
#pragma unroll
      for (int o = 0; o < 3; ++o) {
        if constexpr (K2_PBF16) s += f32x4{__uint_as_float(ph[k][o][0] << 16), __uint_as_float(ph[k][o][0] & 0xffff0000u), __uint_as_float(ph[k][o][1] << 16), __uint_as_float(ph[k][o][1] & 0xffff0000u)};
        else s += __builtin_bit_cast(f32x4, pf[k][o]);
      }
      const unsigned i = 4 * k + kq, u = i / 3, q = i % 3;
      u32x2 o2 = {pk2(s[0], s[1]), pk2(s[2], s[3])};
      if (i < 15) *(u32x2*)(outp + (size_t)(16 * q) * (2 * DM * 2) + (16 * u) * 2) = o2;
    }
    asm volatile("s_waitcnt vmcnt(0)" ::: "memory");
    STAMP2(5);
  }
}

extern "C" void kernel_launch(void* const* d_in, const int* in_sizes, int n_in, void* d_out, int out_size, void* d_ws, size_t ws_size, hipStream_t stream) {
  (void)in_sizes; (void)n_in; (void)out_size; (void)ws_size;
  (void)hipFuncSetAttribute((const void*)k1_attn, hipFuncAttributeMaxDynamicSharedMemorySize, K1_LDS);
  (void)hipFuncSetAttribute((const void*)k2_oproj, hipFuncAttributeMaxDynamicSharedMemorySize, K2_LDS);
  K1Args a;
  a.x[0] = (const char*)d_in[0]; a.k[0] = (const char*)d_in[1]; a.v[0] = (const char*)d_in[2]; a.wq[0] = (const char*)d_in[3];
  a.x[1] = (const char*)d_in[5]; a.k[1] = (const char*)d_in[6]; a.v[1] = (const char*)d_in[7]; a.wq[1] = (const char*)d_in[8];
  a.wo[0] = (const char*)d_in[4]; a.wo[1] = (const char*)d_in[9];
  a.ofrag = (char*)d_ws; a.cnt = (unsigned*)((char*)d_ws + WS_CNT);
  k1_attn<<<256, 768, K1_LDS, stream>>>(a);
  K2Args c;
  c.wo[0] = (const char*)d_in[4]; c.wo[1] = (const char*)d_in[9]; c.ws = (char*)d_ws; c.out = (char*)d_out;
  k2_oproj<<<256, 512 + 64 * K2_NL, K2_LDS, stream>>>(c);
}
```

```cpp
#include <hip/hip_runtime.h>
#include <hip/hip_bf16.h>
#include <stdint.h>
#include <utility>

typedef __bf16 bf16x2 __attribute__((ext_vector_type(2)));
typedef __bf16 bf16x4 __attribute__((ext_vector_type(4)));
typedef __bf16 bf16x8 __attribute__((ext_vector_type(8)));
typedef float f32x2 __attribute__((ext_vector_type(2)));
typedef float f32x4 __attribute__((ext_vector_type(4)));
typedef unsigned u32x2 __attribute__((ext_vector_type(2)));
typedef unsigned u32x4 __attribute__((ext_vector_type(4)));
#define LAS __attribute__((address_space(3)))
#define DI __device__ __forceinline__
typedef __amdgpu_buffer_rsrc_t rsrc_t;

constexpr int NB = 16, SQ = 96, DM = 640, NH = 32, SK = 512, DH = 192, HD = NH * DH;
#define WQ_NT false
#define X_NT false
#define K2_WO_NT false
#define O_NT 1
#define DMA_MODE 1
#define KV_NT true
#define LPRIO 3
#define WARM 1
#define STAG 0
#define STAG_MASK 1
#define W0_NT 0
#define TOFF 26
#define LA 4
#define PRIO_MODE 0
#define QSL 6
#define NUNITS 2
constexpr unsigned TILE = 12288;

template <int... Is, class F> DI void sfor_impl(std::integer_sequence<int, Is...>, F&& f) { (f(std::integral_constant<int, Is>{}), ...); }
template <int N, class F> DI void sfor(F&& f) { sfor_impl(std::make_integer_sequence<int, N>{}, f); }

DI unsigned pk2(float lo, float hi) { f32x2 v = {lo, hi}; bf16x2 b = __builtin_convertvector(v, bf16x2); return __builtin_bit_cast(unsigned, b); }
DI rsrc_t mk_rsrc(const void* p, unsigned bytes) { return __builtin_amdgcn_make_buffer_rsrc((void*)p, (short)0, (int)bytes, 0x00020000); }
template <bool NT> DI void dma16(rsrc_t r, unsigned voff, unsigned soff, unsigned lds) {
  if constexpr (NT) asm volatile("s_mov_b32 m0, %0\n\ts_nop 0\n\tbuffer_load_dwordx4 %1, %2, %3 offen nt lds" ::"s"(lds), "v"(voff), "s"(r), "s"(soff) : "memory");
  else              asm volatile("s_mov_b32 m0, %0\n\ts_nop 0\n\tbuffer_load_dwordx4 %1, %2, %3 offen lds" ::"s"(lds), "v"(voff), "s"(r), "s"(soff) : "memory");
}
#define WARM_NT 0
DI void warm64(rsrc_t r, unsigned voff, unsigned soff, unsigned lds) {
  if (WARM_NT) asm volatile("s_mov_b32 m0, %0\n\ts_nop 0\n\tbuffer_load_dword %1, %2, %3 offen nt lds" ::"s"(lds), "v"(voff), "s"(r), "s"(soff) : "memory");
  else asm volatile("s_mov_b32 m0, %0\n\ts_nop 0\n\tbuffer_load_dword %1, %2, %3 offen lds" ::"s"(lds), "v"(voff), "s"(r), "s"(soff) : "memory");
}
template <int N> DI void wait_bar() { asm volatile("s_waitcnt vmcnt(%0) lgkmcnt(0)\n\ts_barrier" ::"i"(N) : "memory"); }
DI void lgk_bar() {
  asm volatile("" ::: "memory");
  __builtin_amdgcn_s_waitcnt(0xC07F);
  __builtin_amdgcn_s_barrier();
  asm volatile("" ::: "memory");
}
DI bf16x8 lds128(LAS char* L, unsigned a) { return *(LAS bf16x8*)(L + a); }
DI bf16x8 ldstr(LAS char* L, unsigned a0, unsigned a1) {
  bf16x4 lo = __builtin_amdgcn_ds_read_tr16_b64_v4bf16((LAS bf16x4*)(L + a0));
  bf16x4 hi = __builtin_amdgcn_ds_read_tr16_b64_v4bf16((LAS bf16x4*)(L + a1));
  return __builtin_shufflevector(lo, hi, 0, 1, 2, 3, 4, 5, 6, 7);
}
DI float max3f(float a, float b, float c) { return __builtin_elementwise_maximum(__builtin_elementwise_maximum(a, b), c); }
DI float rowred_max(float x) {
  auto a = __builtin_amdgcn_permlane16_swap(__float_as_uint(x), __float_as_uint(x), false, false);
  x = __builtin_elementwise_maximum(__uint_as_float(a[0]), __uint_as_float(a[1]));
  auto b = __builtin_amdgcn_permlane32_swap(__float_as_uint(x), __float_as_uint(x), false, false);
  return __builtin_elementwise_maximum(__uint_as_float(b[0]), __uint_as_float(b[1]));
}
DI float rowred_sum(float x) {
  auto a = __builtin_amdgcn_permlane16_swap(__float_as_uint(x), __float_as_uint(x), false, false);
  x = __uint_as_float(a[0]) + __uint_as_float(a[1]);
  auto b = __builtin_amdgcn_permlane32_swap(__float_as_uint(x), __float_as_uint(x), false, false);
  return __uint_as_float(b[0]) + __uint_as_float(b[1]);
}
#define SB() __builtin_amdgcn_sched_barrier(0)
#define MFMA(a, b, c) __builtin_amdgcn_mfma_f32_16x16x32_bf16(a, b, c, 0, 0, 0)

constexpr size_t OFRAG_UNIT = 36 * 512;

#define STAMP(i)
#define STAMP2(i)
#define WT_BEGIN()
#define WT_END(acc)
#define WT_STORE(i, v)
#define WT_DECL()
struct K1Args { const char* x[2]; const char* k[2]; const char* v[2]; const char* wq[2]; char* ofrag; unsigned* cnt; };

constexpr int NEV = 30;
constexpr int issued(int e) {
  if (e < 0) return 10;
  if (e <= 7) return 13 + 3 * e;
  if (e <= 9) return 37;
  if (e == 10 || e == 11) return 40;
  if (e <= 19) return 40 + 2 * (e - 11);
  if (e == 20) return 56;
  if (e <= 23) return 56 + 2 * (e - 20);
  if (e == 24) return 62;
  if (e == 25) return 65;
  if (e == 26) return 68;
  if (e == 27) return 70;
  return 72;
}
constexpr int kslot(int n) { const int r = n % 10; return r < 4 ? r : (r < 7 ? r + 3 : r - 3); }
constexpr int needpos(int e) {
  if (e < 10) return 3 * e + 2 < 6 ? 3 * e + 2 : 3 * e + 2 + 4;
  if (e >= 11 && e <= 18) { const int n = 2 * (e - 11) + 1; return n < 4 ? 6 + n : 30 + n; }
  if (e >= 20 && e <= 27) return 30 + 17 + 2 * (e - 20);
  return -1;
}
constexpr int imin(int a, int b) { return a < b ? a : b; }
constexpr int allowed(int e, bool last) { return needpos(e) < 0 ? 63 : imin(63, 3 * ((last ? imin(issued(e - 1), 62) : issued(e - 1)) - needpos(e) - 1)); }

constexpr unsigned AUX = 10 * TILE;
constexpr unsigned MISC = 13 * TILE;
constexpr unsigned K1_LDS = 13 * TILE + 4096;

__global__ __launch_bounds__(768, 3) void k1_attn(K1Args A) {
  extern __shared__ __attribute__((aligned(16))) char lds_raw[];
  LAS char* L = (LAS char*)lds_raw;
  const unsigned tid = threadIdx.x, lane = tid & 63;
  const unsigned w12 = __builtin_amdgcn_readfirstlane(tid >> 6);
  const unsigned bid = blockIdx.x, xcd = bid & 7, idx = bid >> 3;
  const unsigned br = xcd >> 2, h = 8 * (xcd & 3) + (idx & 7), b0 = idx >> 3;
  unsigned S[10];
#pragma unroll
  for (int i = 0; i < 10; ++i) S[i] = i * TILE;
  auto rotate = [&]() {
    const unsigned t0 = S[0], t1 = S[1], t2 = S[2], t3 = S[3], t4 = S[4], t5 = S[5], t6 = S[6], t7 = S[7], t8 = S[8], t9 = S[9];
    S[4] = t2; S[5] = t3; S[6] = t7; S[7] = t8; S[8] = t9; S[9] = t4; S[0] = t5; S[1] = t6; S[2] = t0; S[3] = t1;
  };
  auto qslot = [&](int qi) -> unsigned { const int m = qi / 3, r = qi % 3; return m % 3 == 2 ? AUX + r * TILE : S[4 + 3 * (m % 3) + r]; };
  auto olane = [&]() { unsigned l = lane; asm volatile("" : "+v"(l)); return l; };

  if (w12 >= 8) {
    if (LPRIO) __builtin_amdgcn_s_setprio(LPRIO);
    const unsigned w = w12 - 8;
    const unsigned ldsb = (unsigned)(uintptr_t)L;
    if (bid < 64 && tid == 512) A.cnt[bid * 32] = 0;
    const char* xp = br ? A.x[1] : A.x[0];
    const char* wp = br ? A.wq[1] : A.wq[0];
    const char* kbase = (br ? A.k[1] : A.k[0]);
    const char* vbase = (br ? A.v[1] : A.v[0]);
    const rsrc_t rw = mk_rsrc(wp + h * DH * 2, DM * HD * 2 - h * DH * 2);
    unsigned voX[3], voW[3], voK[3], voV[3];
#pragma unroll
    for (int i = 0; i < 3; ++i) {
      const unsigned P = 3 * w + i, row = 8 * P + (lane >> 3);
      voX[i] = row * (DM * 2) + 16 * ((lane & 7) ^ ((row >> 1) & 7));
      const unsigned o = 64 * i + lane, r = 8 * w + o / 24, cp = o % 24;
      const unsigned fW = 2 * (((r >> 1) & 1) | (((r >> 3) & 1) << 1)), fK = (r >> 1) & 7, fV = 2 * ((r >> 1) & 3);
      voW[i] = r * (HD * 2) + 16 * (cp ^ fW);
      voK[i] = r * 384 + 16 * (cp ^ fK);
      voV[i] = r * 384 + 16 * (cp ^ fV);
    }
    const unsigned dst_w = ldsb + 3072 * w;
    rsrc_t rx = mk_rsrc(xp + (size_t)b0 * SQ * DM * 2, SQ * DM * 2);
    rsrc_t rk = mk_rsrc(kbase + (size_t)(b0 * NH + h) * (SK * DH * 2), SK * DH * 2);
    rsrc_t rv = mk_rsrc(vbase + (size_t)(b0 * NH + h) * (SK * DH * 2), SK * DH * 2);
    unsigned soX = 0, soW = 0, soK = 0, soV = 0;
    auto issueQ = [&](rsrc_t rxx, int i, unsigned slot) {
      if (i % 3 == 1) {
#pragma unroll
        for (int p = 0; p < 3; ++p) dma16<X_NT>(rxx, voX[p], soX, dst_w + slot + 1024 * p);
        soX += 128;
      } else {
#pragma unroll
        for (int p = 0; p < 3; ++p) dma16<WQ_NT>(rw, voW[p], soW, dst_w + slot + 1024 * p);
        soW += 32u * HD * 2;
      }
    };
    auto issueK = [&](rsrc_t rkk, unsigned slot) {
#pragma unroll
      for (int p = 0; p < 3; ++p) dma16<KV_NT>(rkk, voK[p], soK, dst_w + slot + 1024 * p);
      soK += TILE;
    };
    auto issueV = [&](unsigned slot) {
#pragma unroll
      for (int p = 0; p < 3; ++p) dma16<KV_NT>(rv, voV[p], soV, dst_w + slot + 1024 * p);
      soV += TILE;
    };
    if (STAG > 0) {
      const unsigned long long t0 = __builtin_amdgcn_s_memrealtime(), dl = (unsigned long long)STAG * (b0 & STAG_MASK);
      while (__builtin_amdgcn_s_memrealtime() - t0 < dl) __builtin_amdgcn_s_sleep(8);
    }
#pragma unroll
    for (int i = 0; i < 6; ++i) issueQ(rx, i, S[4 + i]);
#pragma unroll
    for (int n = 0; n < 4; ++n) issueK(rk, S[n]);
    for (int un = 0; un < 4; ++un) {
      const bool last = un == 3;
      const unsigned bn = last ? b0 : b0 + 4 * (un + 1);
      const rsrc_t rxn = mk_rsrc(xp + (size_t)bn * SQ * DM * 2, SQ * DM * 2);
      const rsrc_t rkn = mk_rsrc(kbase + (size_t)(bn * NH + h) * (SK * DH * 2), SK * DH * 2);
      const rsrc_t rvn = mk_rsrc(vbase + (size_t)(bn * NH + h) * (SK * DH * 2), SK * DH * 2);
      sfor<NEV>([&](auto E) {
        constexpr int e = E;
        if constexpr (allowed(e, false) == allowed(e, true)) wait_bar<allowed(e, false)>();
        else { if (last) wait_bar<allowed(e, true)>(); else wait_bar<allowed(e, false)>(); }
        constexpr int lo = issued(e - 1), hi = issued(e);
        sfor<3>([&](auto D) {
          constexpr int pos = lo + D;
          if constexpr (pos < hi) {
            if constexpr (pos < 34) {
              constexpr int qi = pos - 4;
              issueQ(rx, qi, qslot(qi));
            } else if constexpr (pos < 62) {
              constexpr int n = pos - 30;
              if constexpr (n < 16) issueK(rk, S[kslot(n)]); else issueV(S[kslot(n)]);
            } else if (!last) {
              if constexpr (pos < 68) {
                if constexpr (pos == 62) { soX = 0; soW = 0; asm volatile("" : "+s"(soX), "+s"(soW)); }
                issueQ(rxn, pos - 62, S[kslot(22 + pos - 62)]);
              } else {
                if constexpr (pos == 68) { soK = 0; asm volatile("" : "+s"(soK)); }
                issueK(rkn, S[kslot(28 + pos - 68)]);
              }
            }
          }
        });
      });
      rotate();
      rx = rxn; rk = rkn; rv = rvn;
      soV = 0; asm volatile("" : "+s"(soV));
    }
    return;
  }

  const unsigned cw = w12, qh = cw >> 2, jj = cw & 3;
  const unsigned wscr = (unsigned)(uintptr_t)L + MISC + 3072 + 128 * 0;
  auto warm_x = [&](unsigned bb) {
    if (WARM && cw < 2) {
      const rsrc_t r = mk_rsrc((br ? A.x[1] : A.x[0]) + (size_t)bb * SQ * DM * 2, SQ * DM * 2);
      const unsigned l = olane(), line = 120 * (idx & 7) + 60 * cw + (l < 60 ? l : 59);
      warm64(r, line * 128, 0, wscr);
    }
  };
  if (WARM) {
    const rsrc_t r = mk_rsrc((br ? A.wq[1] : A.wq[0]) + h * DH * 2, DM * HD * 2 - h * DH * 2);
    const unsigned l = olane(), i = 60 * cw + (l < 60 ? l : 59), row = 160 * b0 + i / 3;
    warm64(r, row * (HD * 2) + 128 * (i % 3), 0, wscr);
    warm_x(b0);
  }
  const float C = 0.07216878364870322f * 1.4426950408889634f;
  LAS float* red = (LAS float*)(L + MISC);
  for (int un = 0; un < 4; ++un) {
    const unsigned b = b0 + 4 * un;
    STAMP(0);
    unsigned bW[3], bX0;
    {
      const unsigned l = olane(), c = l & 15, gg = l >> 4, a = c >> 2, p = l & 3;
      const unsigned r = 8 * gg + a, fW = 2 * (((r >> 1) & 1) | (((r >> 3) & 1) << 1));
#pragma unroll
      for (int e = 0; e < 3; ++e) { const unsigned ch = 2 * (3 * jj + e) + (p >> 1); bW[e] = 384 * r + 128 * (ch >> 3) + 16 * ((ch & 7) ^ fW) + 8 * (p & 1); }
      bX0 = 128 * (48 * qh + c) + 16 * (gg ^ ((c >> 1) & 7));
    }
    f32x4 qacc[3][3];
#pragma unroll
    for (int e = 0; e < 3; ++e)
#pragma unroll
      for (int t = 0; t < 3; ++t) qacc[e][t] = f32x4{0, 0, 0, 0};
    bf16x8 xd[3], wd[2];
    auto q_tail = [&]() {
#pragma unroll
      for (int f = 4; f < 6; ++f)
#pragma unroll
        for (int t = 0; t < 3; ++t) qacc[f % 3][t] = MFMA(wd[f - 4], xd[t], qacc[f % 3][t]);
    };
    sfor<10>([&](auto M) {
      constexpr int m = M;
      lgk_bar();
      const unsigned sw0 = qslot(3 * m), sx = qslot(3 * m + 1), sw1 = qslot(3 * m + 2);
      bf16x8 xf[3], wf[4], xn[3], wn[2];
      auto rdW = [&](int f) { const unsigned a = bW[f % 3] + (f < 3 ? sw0 : sw1); return ldstr(L, a, a + 1536); };
      wf[0] = rdW(0);
#pragma unroll
      for (int t = 0; t < 3; ++t) xf[t] = lds128(L, bX0 + sx + 2048 * t);
      wf[1] = rdW(1);
      SB();
      if constexpr (m > 0) q_tail();
      SB();
      sfor<4>([&](auto F) {
        constexpr int f = F;
        if constexpr (f + 2 < 4) wf[f + 2] = rdW(f + 2); else wn[f - 2] = rdW(f + 2);
        if constexpr (f == 0) {
#pragma unroll
          for (int t = 0; t < 3; ++t) xn[t] = lds128(L, (bX0 ^ 64) + sx + 2048 * t);
        }
#pragma unroll
        for (int t = 0; t < 3; ++t) qacc[f % 3][t] = MFMA(wf[f], f < 3 ? xf[t] : xn[t], qacc[f % 3][t]);
        SB();
      });
#pragma unroll
      for (int t = 0; t < 3; ++t) xd[t] = xn[t];
      wd[0] = wn[0]; wd[1] = wn[1];
    });
    q_tail();
    STAMP(1);
    bf16x8 qf[6][3];
    unsigned bK0;
    {
      const unsigned l = olane(), c = l & 15, gg = l >> 4;
      const unsigned fK = (c >> 1) & 7;
      bK0 = 384 * c + 16 * (gg ^ fK);
#pragma unroll
      for (int e = 0; e < 3; ++e) {
        const unsigned ch = 2 * (3 * jj + e) + (gg >> 1);
        const unsigned a = AUX + 18432 * qh + 384 * c + 128 * (ch >> 3) + 16 * ((ch & 7) ^ fK) + 8 * (gg & 1);
#pragma unroll
        for (int t = 0; t < 3; ++t)
          *(LAS u32x2*)(L + a + 6144 * t) = u32x2{pk2(qacc[e][t][0], qacc[e][t][1]), pk2(qacc[e][t][2], qacc[e][t][3])};
      }
      lgk_bar();
#pragma unroll
      for (int s = 0; s < 6; ++s)
#pragma unroll
        for (int t = 0; t < 3; ++t) qf[s][t] = lds128(L, AUX + 18432 * qh + 6144 * t + ((s & 1) ? (bK0 ^ 64) : bK0) + 128 * (s >> 1));
    }
    STAMP(2);
    if (un < 3) warm_x(b + 4);
    u32x2 sp[8][3];
    float mx[3] = {-3.0e38f, -3.0e38f, -3.0e38f};
    f32x4 sa[3];
    bf16x8 kd[2];
    auto k_tail = [&](auto T) {
      constexpr int t = T;
#pragma unroll
      for (int s = 4; s < 6; ++s)
#pragma unroll
        for (int q = 0; q < 3; ++q) sa[q] = MFMA(kd[s - 4], qf[s][q], sa[q]);
#pragma unroll
      for (int q = 0; q < 3; ++q) {
        mx[q] = max3f(max3f(sa[q][0], sa[q][1], mx[q]), sa[q][2], sa[q][3]);
        sp[t][q] = u32x2{pk2(sa[q][0], sa[q][1]), pk2(sa[q][2], sa[q][3])};
        asm volatile("" : "+v"(sp[t][q]), "+v"(mx[q]));
      }
    };
    sfor<8>([&](auto T) {
      constexpr int t = T;
      lgk_bar();
      const unsigned so = (jj >> 1) ? S[kslot(2 * t + 1)] : S[kslot(2 * t)];
      const unsigned k0 = bK0 + 6144 * (jj & 1) + so, k1 = k0 ^ 64;
      bf16x8 kf[4], kn[2];
      auto rdK = [&](int s) { return lds128(L, ((s & 1) ? k1 : k0) + 128 * (s >> 1)); };
      kf[0] = rdK(0); kf[1] = rdK(1);
      SB();
      if constexpr (t > 0) k_tail(std::integral_constant<int, (t > 0 ? t - 1 : 0)>{});
      SB();
#pragma unroll
      for (int q = 0; q < 3; ++q) sa[q] = f32x4{0, 0, 0, 0};
      sfor<4>([&](auto F) {
        constexpr int s = F;
        if constexpr (s + 2 < 4) kf[s + 2] = rdK(s + 2); else kn[s - 2] = rdK(s + 2);
#pragma unroll
        for (int q = 0; q < 3; ++q) sa[q] = MFMA(kf[s], qf[s][q], sa[q]);
        SB();
      });
      kd[0] = kn[0]; kd[1] = kn[1];
    });
    k_tail(std::integral_constant<int, 7>{});
    STAMP(3);
    const unsigned lsm = olane(), csm = lsm & 15;
#pragma unroll
    for (int q = 0; q < 3; ++q) {
      mx[q] = rowred_max(mx[q]);
      if (lsm < 16) red[cw * 48 + 16 * q + csm] = mx[q];
    }
    lgk_bar();
    float mc[3], s0[3], s1[3];
    auto expo = [&](int t, int q, int r) {
      const unsigned u = sp[t][q][r];
      const float p0 = __builtin_amdgcn_exp2f(fmaf(__uint_as_float(u << 16), C, mc[q]));
      const float p1 = __builtin_amdgcn_exp2f(fmaf(__uint_as_float(u & 0xffff0000u), C, mc[q]));
      s0[q] += p0; s1[q] += p1;
      asm volatile("" : "+v"(s0[q]), "+v"(s1[q]));
      sp[t][q][r] = pk2(p0, p1);
    };
    unsigned bV[3], pw, pr;
    {
      const unsigned gg = lsm >> 4, a = csm >> 2, p = lsm & 3;
      const unsigned rv_ = 4 * gg + a, fV = 2 * ((rv_ >> 1) & 3);
#pragma unroll
      for (int e = 0; e < 3; ++e) { const unsigned ch = 2 * (3 * jj + e) + (p >> 1); bV[e] = 384 * rv_ + 128 * (ch >> 3) + 16 * ((ch & 7) ^ fV) + 8 * (p & 1); }
      pr = AUX + 6144 * qh + 16 * lsm;
      pw = pr + 3072 * (jj >> 1) + 8 * (jj & 1);
    }
#pragma unroll
    for (int q = 0; q < 3; ++q) {
      const unsigned ri = 4 * qh * 48 + 16 * q + csm;
      const float m = max3f(max3f(red[ri], red[ri + 48], red[ri + 96]), red[ri + 144], mx[q]);
      mc[q] = -m * C; s0[q] = 0.f; s1[q] = 0.f;
      expo(0, q, 0); expo(0, q, 1);
      *(LAS u32x2*)(L + pw + 1024 * q) = sp[0][q];
    }
    STAMP(4);
    f32x4 oacc[3][3];
#pragma unroll
    for (int e = 0; e < 3; ++e)
#pragma unroll
      for (int t = 0; t < 3; ++t) oacc[e][t] = f32x4{0, 0, 0, 0};
    bf16x8 pd[3], vd[2];
    auto v_tail = [&]() {
#pragma unroll
      for (int f = 4; f < 6; ++f)
#pragma unroll
        for (int q = 0; q < 3; ++q) oacc[f % 3][q] = MFMA(vd[f - 4], pd[q], oacc[f % 3][q]);
    };
    sfor<8>([&](auto T) {
      constexpr int t = T, n = 16 + 2 * t;
      lgk_bar();
      bf16x8 pf[3], pn[3], vf[4], vn[2];
      auto rdV = [&](int f) { const unsigned a = bV[f % 3] + S[kslot(n + f / 3)]; return ldstr(L, a, a + 6144); };
#pragma unroll
      for (int q = 0; q < 3; ++q) pf[q] = lds128(L, pr + 12288 * (t & 1) + 1024 * q);
      vf[0] = rdV(0); vf[1] = rdV(1);
      SB();
      if constexpr (t > 0) v_tail();
      if constexpr (t < 7) { expo(t + 1, 0, 0); expo(t + 1, 0, 1); *(LAS u32x2*)(L + pw + 12288 * ((t + 1) & 1)) = sp[t + 1][0]; }
      SB();
      sfor<4>([&](auto F) {
        constexpr int f = F;
        if constexpr (f + 2 < 4) vf[f + 2] = rdV(f + 2); else vn[f - 2] = rdV(f + 2);
        if constexpr (f == 0) {
#pragma unroll
          for (int q = 0; q < 3; ++q) pn[q] = lds128(L, pr + 12288 * (t & 1) + 3072 + 1024 * q);
        }
#pragma unroll
        for (int q = 0; q < 3; ++q) oacc[f % 3][q] = MFMA(vf[f], f < 3 ? pf[q] : pn[q], oacc[f % 3][q]);
        if constexpr (t < 7) {
          expo(t + 1, 1 + (f >> 1), f & 1);
          if constexpr (f & 1) *(LAS u32x2*)(L + pw + 12288 * ((t + 1) & 1) + 1024 * (1 + (f >> 1))) = sp[t + 1][1 + (f >> 1)];
        }
        SB();
      });
#pragma unroll
      for (int q = 0; q < 3; ++q) pd[q] = pn[q];
      vd[0] = vn[0]; vd[1] = vn[1];
    });
    v_tail();
    STAMP(5);
    float ls[3];
    const unsigned l = olane();
#pragma unroll
    for (int q = 0; q < 3; ++q) {
      const float s = rowred_sum(s0[q] + s1[q]);
      if (l < 16) red[384 + cw * 48 + 16 * q + l] = s;
    }
    lgk_bar();
#pragma unroll
    for (int q = 0; q < 3; ++q) {
      const unsigned ri = 384 + 4 * qh * 48 + 16 * q + (l & 15);
      ls[q] = __builtin_amdgcn_rcpf((red[ri] + red[ri + 48]) + (red[ri + 96] + red[ri + 144]));
    }
#pragma unroll
    for (int e = 0; e < 3; ++e) {
      const unsigned u = 3 * jj + e;
      const unsigned a = AUX + ((u >> 1) * 6 + 3 * qh) * 1024 + 16 * l + 8 * (u & 1);
#pragma unroll
      for (int q = 0; q < 3; ++q)
        *(LAS u32x2*)(L + a + 1024 * q) = u32x2{pk2(oacc[e][q][0] * ls[q], oacc[e][q][1] * ls[q]), pk2(oacc[e][q][2] * ls[q], oacc[e][q][3] * ls[q])};
    }
    lgk_bar();
    {
      char* ob = A.ofrag + ((size_t)((br * 16 + b) * NH + h) * OFRAG_UNIT) * 2 + 16 * l + 1024 * cw;
      u32x4 o[5];
#pragma unroll
      for (int i = 0; i < 5; ++i) if (i < 4 || cw < 4) o[i] = *(LAS u32x4*)(L + AUX + 1024 * cw + 8192 * i + 16 * l);
#pragma unroll
      for (int i = 0; i < 5; ++i) if (i < 4 || cw < 4) {
        if (O_NT) __builtin_nontemporal_store(o[i], (u32x4*)(ob + 8192 * i)); else *(u32x4*)(ob + 8192 * i) = o[i];
      }
    }
    rotate();
    STAMP(6);
  }
}

#define K2_NBUF 4
#define K2_WARM 0
#define K2_PBF16 1
constexpr unsigned K2_STAGE = 45056;
constexpr unsigned K2_LDS = 3 * K2_STAGE + 256;
constexpr size_t WS_PART = (size_t)32 * 32 * OFRAG_UNIT * 2;
constexpr size_t PART_BYTES = (size_t)64 * 4 * 8 * 15 * 1024;
constexpr size_t WS_CNT = WS_PART + PART_BYTES;
struct K2Args { const char* wo[2]; char* ws; char* out; };
typedef __attribute__((address_space(1))) unsigned gu32;

#define K2_NL 4
__global__ __launch_bounds__(512 + 64 * K2_NL, 2 + K2_NL / 4) void k2_oproj(K2Args A) {
  extern __shared__ __attribute__((aligned(16))) char lds_raw[];
  LAS char* L = (LAS char*)lds_raw;
  const unsigned ldsb = (unsigned)(uintptr_t)L;
  const unsigned tid = threadIdx.x, lane = tid & 63;
  const unsigned w = __builtin_amdgcn_readfirstlane(tid >> 6);
  const unsigned mh = (w >> 2) & 1, nh = (w >> 1) & 1, qh = w & 1;
  const unsigned c16 = lane & 15, g = lane >> 4, a4 = c16 >> 2, p4 = lane & 3;
  const unsigned bid = blockIdx.x, xcd = bid & 7, idx = bid >> 3;
  const unsigned br = xcd >> 2, kq = xcd & 3, mb = idx >> 2, nb = idx & 3;
  const char* wo = (br ? A.wo[1] : A.wo[0]);
  STAMP2(0);
  f32x4 acc[5][3];
#pragma unroll
  for (int u = 0; u < 5; ++u)
#pragma unroll
    for (int q = 0; q < 3; ++q) acc[u][q] = f32x4{0, 0, 0, 0};
  if (w >= 8) {
    if (LPRIO) __builtin_amdgcn_s_setprio(LPRIO);
    const unsigned lw = w - 8;
    constexpr int NO = 24 / K2_NL, NWP = K2_NL == 4 ? 5 : 3;
    const unsigned go = br * 16 + 2 * mb + (lw * NO) / 12;
    const rsrc_t ro = mk_rsrc(A.ws + ((size_t)(go * NH + 8 * kq) * OFRAG_UNIT) * 2, 8 * OFRAG_UNIT * 2);
    const rsrc_t rw = mk_rsrc(wo + (size_t)(8 * kq * DH) * (DM * 2) + nb * 320, 8 * DH * DM * 2 - nb * 320);
    const unsigned voO = 16 * lane + 1024 * ((lw * NO) % 12), dstO = ldsb + 1024 * NO * lw;
    const unsigned pW0 = K2_NL == 4 ? 5 * lw : (lw < 4 ? 3 * lw : 12 + 2 * (lw - 4));
    unsigned voW[NWP];
#pragma unroll
    for (int p = 0; p < NWP; ++p) { const unsigned o = 64 * (pW0 + p) + lane, r = o / 20, cp = o % 20; voW[p] = r * (DM * 2) + 16 * (cp ^ (2 * ((r >> 2) & 1))); }
    const unsigned dstW = ldsb + 24576 + 1024 * pW0;
    unsigned soO = 0, soW = 0;
    auto stage_dma = [&](unsigned stg) {
#pragma unroll
      for (int p = 0; p < NO; ++p) dma16<false>(ro, voO + 1024 * p, soO, dstO + stg + 1024 * p);
#pragma unroll
      for (int p = 0; p < NWP; ++p) if (K2_NL == 4 || p < 2 || lw < 4) dma16<K2_WO_NT>(rw, voW[p], soW, dstW + stg + 1024 * p);
      soO += 12288; soW += 64 * DM * 2;
    };
    stage_dma(0); stage_dma(K2_STAGE); stage_dma(2 * K2_STAGE);
    unsigned free_stg = 0;
    constexpr int NP = NO + NWP;
    const bool shortw = K2_NL == 8 && lw >= 4;
    for (int st = 0; st < 24; ++st) {
      if (st == 0) { if (shortw) wait_bar<2 * (NP - 1)>(); else wait_bar<2 * NP>(); }
      else if (st < 23) { if (shortw) wait_bar<NP - 1>(); else wait_bar<NP>(); }
      else wait_bar<0>();
      if (st >= 1 && st + 2 < 24) { stage_dma(free_stg); free_stg = (free_stg == 2 * K2_STAGE) ? 0 : free_stg + K2_STAGE; }
    }
    __builtin_amdgcn_s_barrier();
  } else {
    const unsigned bB = 12288 * mh + 3072 * qh + 16 * lane;
    unsigned bA[2];
    {
      const unsigned r = 4 * g + a4, sgn = 32 * (g & 1);
      const unsigned be = 24576 + 320 * r + 8 * p4 + 160 * nh + sgn, bo = 24576 + 320 * r + 8 * p4 + 160 * nh - sgn;
      bA[0] = nh ? bo : be; bA[1] = nh ? be : bo;
    }
    u32x4 da[5], db[3];
    unsigned stc = 0;
    auto mm = [&](u32x4 (&fa)[5], u32x4 (&fb)[3]) {
#pragma unroll
      for (int u = 0; u < 5; ++u)
#pragma unroll
        for (int q = 0; q < 3; ++q) acc[u][q] = MFMA(__builtin_bit_cast(bf16x8, fa[u]), __builtin_bit_cast(bf16x8, fb[q]), acc[u][q]);
    };
    auto rd = [&](int i, u32x4 (&fa)[5], u32x4 (&fb)[3], unsigned ob, unsigned oa) {
      if (i == 0 || i == 2 || i == 3) { const int q = i == 0 ? 0 : i - 1; fb[q] = __builtin_bit_cast(u32x4, lds128(L, bB + ob + 1024 * q)); }
      else { const int u = i == 1 ? 0 : i - 3; const unsigned a = ((u & 1) ? bA[1] : bA[0]) + oa + 32 * u; fa[u] = __builtin_bit_cast(u32x4, ldstr(L, a, a + 5120)); }
    };
    auto half = [&](bool domm, u32x4 (&ma)[5], u32x4 (&mb_)[3], u32x4 (&ra)[5], u32x4 (&rb)[3], unsigned ob, unsigned oa) {
#pragma unroll
      for (int i = 0; i < 15; ++i) {
        if (domm) acc[i / 3][i % 3] = MFMA(__builtin_bit_cast(bf16x8, ma[i / 3]), __builtin_bit_cast(bf16x8, mb_[i % 3]), acc[i / 3][i % 3]);
        if (i < 8) rd(i, ra, rb, ob, oa);
        SB();
      }
    };
    for (int st = 0; st < 24; ++st) {
      lgk_bar();
      u32x4 fa[5], fb[3];
      half(st > 0, da, db, fa, fb, stc, stc);
      __builtin_amdgcn_s_waitcnt(0xC07F);
      SB();
      half(true, fa, fb, da, db, stc + 6144, stc + 10240);
      stc = (stc == 2 * K2_STAGE) ? 0 : stc + K2_STAGE;
    }
    lgk_bar();
    mm(da, db);
  }
  STAMP2(1);
  STAMP2(2);
  {
    const unsigned tg = (br * 8 + mb) * 4 + nb;
    constexpr unsigned TB = K2_PBF16 ? 512 : 1024;
    char* pbase = A.ws + WS_PART + (size_t)tg * (4 * 8 * 15 * TB);
    const rsrc_t rp = mk_rsrc(pbase, 4 * 8 * 15 * TB);
    const unsigned vo = (w * 15) * TB + (TB / 64) * lane;
#pragma unroll
    for (int i = 0; i < 15; ++i)
      if (w < 8 && (i & 3) != kq) {
        const f32x4 v = acc[i / 3][i % 3];
        if constexpr (K2_PBF16) __builtin_amdgcn_raw_buffer_store_b64(u32x2{pk2(v[0], v[1]), pk2(v[2], v[3])}, rp, vo + TB * i, kq * (8 * 15 * TB), 16);
        else __builtin_amdgcn_raw_buffer_store_b128(__builtin_bit_cast(u32x4, v), rp, vo + TB * i, kq * (8 * 15 * TB), 16);
      }
    asm volatile("s_waitcnt vmcnt(0)" ::: "memory");
    __builtin_amdgcn_s_barrier();
    STAMP2(3);
    gu32* cnt = (gu32*)(A.ws + WS_CNT + 128 * tg);
    if (tid == 0) {
      __hip_atomic_fetch_add(cnt, 1u, __ATOMIC_RELAXED, __HIP_MEMORY_SCOPE_AGENT);
      unsigned spins = 0;
      while (__hip_atomic_load(cnt, __ATOMIC_RELAXED, __HIP_MEMORY_SCOPE_AGENT) < 4u) { __builtin_amdgcn_s_sleep(2); if (++spins > (1u << 22)) break; }
    }
    asm volatile("s_waitcnt vmcnt(0)" ::: "memory");
    __builtin_amdgcn_s_barrier();
    asm volatile("" ::: "memory");
    STAMP2(4);
    if (w >= 8) return;
    const unsigned b = 2 * mb + mh;
    char* outp = A.out + ((size_t)(b * SQ + 48 * qh + c16) * (2 * DM) + br * DM + nb * 160 + nh * 80 + 4 * g) * 2;
    f32x4 own[4];
    u32x4 pf[4][3];
    u32x2 ph[4][3];
#pragma unroll
    for (int k = 0; k < 4; ++k) {
      own[k] = kq == 0 ? acc[(4 * k) / 3][(4 * k) % 3] : kq == 1 ? acc[(4 * k + 1) / 3][(4 * k + 1) % 3] : kq == 2 ? acc[(4 * k + 2) / 3][(4 * k + 2) % 3]
                                                                                                          : acc[(k < 3 ? 4 * k + 3 : 0) / 3][(k < 3 ? 4 * k + 3 : 0) % 3];
      const unsigned i = 4 * k + kq;
#pragma unroll
      for (int o = 0; o < 3; ++o) {
        const unsigned src = (kq + 1 + o) & 3;
        const unsigned off = i < 15 ? vo + TB * i : 4 * 8 * 15 * TB;
        if constexpr (K2_PBF16) ph[k][o] = __builtin_amdgcn_raw_buffer_load_b64(rp, off, src * (8 * 15 * TB), 16);
        else pf[k][o] = __builtin_amdgcn_raw_buffer_load_b128(rp, off, src * (8 * 15 * TB), 16);
      }
    }
#pragma unroll
    for (int k = 0; k < 4; ++k) {
      f32x4 s = own[k];
#pragma unroll
      for (int o = 0; o < 3; ++o) {
        if constexpr (K2_PBF16) s += f32x4{__uint_as_float(ph[k][o][0] << 16), __uint_as_float(ph[k][o][0] & 0xffff0000u), __uint_as_float(ph[k][o][1] << 16), __uint_as_float(ph[k][o][1] & 0xffff0000u)};
        else s += __builtin_bit_cast(f32x4, pf[k][o]);
      }
      const unsigned i = 4 * k + kq, u = i / 3, q = i % 3;
      u32x2 o2 = {pk2(s[0], s[1]), pk2(s[2], s[3])};
      if (i < 15) *(u32x2*)(outp + (size_t)(16 * q) * (2 * DM * 2) + (16 * u) * 2) = o2;
    }
    asm volatile("s_waitcnt vmcnt(0)" ::: "memory");
    STAMP2(5);
  }
}

extern "C" void kernel_launch(void* const* d_in, const int* in_sizes, int n_in, void* d_out, int out_size, void* d_ws, size_t ws_size, hipStream_t stream) {
  (void)in_sizes; (void)n_in; (void)out_size; (void)ws_size;
  (void)hipFuncSetAttribute((const void*)k1_attn, hipFuncAttributeMaxDynamicSharedMemorySize, K1_LDS);
  (void)hipFuncSetAttribute((const void*)k2_oproj, hipFuncAttributeMaxDynamicSharedMemorySize, K2_LDS);
  K1Args a;
  a.x[0] = (const char*)d_in[0]; a.k[0] = (const char*)d_in[1]; a.v[0] = (const char*)d_in[2]; a.wq[0] = (const char*)d_in[3];
  a.x[1] = (const char*)d_in[5]; a.k[1] = (const char*)d_in[6]; a.v[1] = (const char*)d_in[7]; a.wq[1] = (const char*)d_in[8];
  a.ofrag = (char*)d_ws; a.cnt = (unsigned*)((char*)d_ws + WS_CNT);
  k1_attn<<<256, 768, K1_LDS, stream>>>(a);
  K2Args c;
  c.wo[0] = (const char*)d_in[4]; c.wo[1] = (const char*)d_in[9]; c.ws = (char*)d_ws; c.out = (char*)d_out;
  k2_oproj<<<256, 512 + 64 * K2_NL, K2_LDS, stream>>>(c);
}
```
